```python
import torch

def _softmax(x, axis=-1):
    return torch.softmax(x, dim=axis)
def _gelu(x):
    return torch.nn.functional.gelu(x, approximate="tanh")
def _silu(x):
    return torch.nn.functional.silu(x)
def _sigmoid(x):
    return torch.sigmoid(x)
def _layernorm(x, axis=-1, eps=1e-5):
    m = x.mean(dim=axis, keepdim=True)
    v = x.var(dim=axis, keepdim=True, unbiased=False)
    return (x - m) / torch.sqrt(v + eps)
def _scatter_add(tgt, idx, src, axis=0):
    return tgt.clone().index_add_(axis, idx, src)
def _segment_sum(src, seg, n):
    out = torch.zeros((n,) + src.shape[1:], dtype=src.dtype)
    return out.index_add_(0, seg, src)
def _pad(x, pw):
    flat = tuple(v for pair in reversed(pw) for v in pair)
    return torch.nn.functional.pad(x, flat)

kernel_name = 'standalone__low_rank_proj_linformer__sampling'

def setup_inputs():

    _w = (lambda a: (lambda t: t.to(torch.bfloat16) if t.is_floating_point() else t)(torch.as_tensor(a)))
    B, H, Sq, Skv, Dh, R = 8, 32, 16, 4096, 192, 8
    return {
        "q": _w((lambda *s: torch.randn(*s, dtype=torch.bfloat16) * 0.8)(B, H, Sq, Dh)),
        "k": _w((lambda *s: torch.randn(*s, dtype=torch.bfloat16) * 0.8)(B, H, Skv, Dh)),
        "v": _w((lambda *s: torch.randn(*s, dtype=torch.bfloat16) * 0.05)(B, H, Skv, Dh)),
        "e_proj": _w((lambda *s: torch.randn(*s, dtype=torch.bfloat16) * 0.8)(R, Skv) * (Skv ** -0.5)),
        "f_proj": _w((lambda *s: torch.randn(*s, dtype=torch.bfloat16) * 0.05)(R, Skv) * (Skv ** -0.5)),
    }

def reference(q, k, v, e_proj, f_proj):
    B, H, Sq, Dh = q.shape
    kp = torch.einsum("rn,bhnd->bhrd", e_proj, k)
    vp = torch.einsum("rn,bhnd->bhrd", f_proj, v)
    scores = torch.einsum("bhqd,bhrd->bhqr", q, kp) * (Dh ** -0.5)
    p = _softmax(scores, axis=-1)
    o = torch.einsum("bhqr,bhrd->bhqd", p, vp)
    return o
```

```cpp
#include <hip/hip_runtime.h>
#include <hip/hip_bf16.h>

#define LAS __attribute__((address_space(3)))
typedef unsigned v4u __attribute__((ext_vector_type(4)));
typedef unsigned v2u __attribute__((ext_vector_type(2)));
typedef float f32x4 __attribute__((ext_vector_type(4)));
typedef float f32x2 __attribute__((ext_vector_type(2)));
typedef __bf16 bf16x8 __attribute__((ext_vector_type(8)));
typedef __bf16 bf16x2 __attribute__((ext_vector_type(2)));

constexpr int SQ = 16, SKV = 4096, DH = 192, RK = 8, NBH = 256;
constexpr int NW = 4;
constexpr int TROWS = 64;
constexpr int TILE_BYTES = TROWS * DH * 2;
constexpr int NTILE = SKV / TROWS;
constexpr int TPW = NTILE / NW;
constexpr size_t HEAD_BYTES = (size_t)SKV * DH * 2;
constexpr int PSTR = 196;
constexpr int L_KP = 0;
constexpr int L_VP = L_KP + 8 * 8 * PSTR * 4;
constexpr int VSTR = 200;
constexpr int L_P = L_VP + 8 * 2 * VSTR * 16;
constexpr int L_CNT = L_P + 256;
constexpr int L_END = L_CNT + 16;
constexpr int LDS_BYTES = L_END > 96 * 1024 ? L_END : 96 * 1024;

__device__ __forceinline__ f32x4 mfma32(v4u a, v4u b, f32x4 c) {
  return __builtin_amdgcn_mfma_f32_16x16x32_bf16(__builtin_bit_cast(bf16x8, a), __builtin_bit_cast(bf16x8, b), c, 0, 0, 0);
}
__device__ __forceinline__ unsigned pk_bf16(float lo, float hi) {
  f32x2 v = {lo, hi};
  return __builtin_bit_cast(unsigned, __builtin_convertvector(v, bf16x2));
}
__device__ __forceinline__ float rbf(float x) {
  return (float)(__bf16)x;
}
template <int CTRL> __device__ __forceinline__ float dpp(float x) {
  return __builtin_bit_cast(float, __builtin_amdgcn_mov_dpp(__builtin_bit_cast(int, x), CTRL, 0xf, 0xf, true));
}

__global__ void __launch_bounds__(NW * 64, 1)
linformer_kernel(const char* __restrict__ Q, const char* __restrict__ K, const char* __restrict__ V,
                 const char* __restrict__ E, const char* __restrict__ F, char* __restrict__ O) {
  extern __shared__ __attribute__((aligned(16))) char smem[];
  LAS char* L = (LAS char*)smem;
  const int bh = blockIdx.x;
  const int lane = threadIdx.x & 63, wave = __builtin_amdgcn_readfirstlane(threadIdx.x >> 6);
  const int sg = lane >> 3, c8 = lane & 7;
  const int vo = sg * (8 * DH * 2) + c8 * 16;
  const int eo = c8 * (SKV * 2) + sg * 16;
  const int rot = bh * 5 + wave;
  const char* Kh = K + (size_t)bh * HEAD_BYTES;
  const char* Vh = V + (size_t)bh * HEAD_BYTES;

  auto drs = [&](int i) {
    const int t = (rot + NW * i) & (NTILE - 1);
    const char* b = (i < TPW ? Kh : Vh) + (size_t)t * TILE_BYTES;
    return __builtin_amdgcn_make_buffer_rsrc((void*)b, 0, i < 2 * TPW ? TILE_BYTES : 0, 0x00020000);
  };
  auto ers = [&](int i) {
    const int t = (rot + NW * i) & (NTILE - 1);
    const char* b = (i < TPW ? E : F) + t * (TROWS * 2);
    return __builtin_amdgcn_make_buffer_rsrc((void*)b, 0, i < 2 * TPW ? RK * SKV * 2 - t * (TROWS * 2) : 0, 0x00020000);
  };

  v4u ring[24];
  v4u an;
  {
    const __amdgpu_buffer_rsrc_t r0 = drs(0);
    an = __builtin_amdgcn_raw_buffer_load_b128(ers(0), eo, 0, 0);
    __builtin_amdgcn_sched_barrier(0);
#pragma unroll
    for (int j = 0; j < 24; ++j) {
      ring[j] = __builtin_amdgcn_raw_buffer_load_b128(r0, vo, (j & 7) * 384 + (j >> 3) * 128, 2);
      __builtin_amdgcn_sched_barrier(0);
    }
  }
  v4u qf[6];
  {
    const char* qp = Q + ((size_t)bh * SQ + (lane & 15)) * (DH * 2) + (lane >> 4) * 16;
#pragma unroll
    for (int s = 0; s < 6; ++s) qf[s] = *(const v4u*)(qp + s * 64);
  }
  __builtin_amdgcn_sched_barrier(0);
  if (threadIdx.x == 0) *(LAS unsigned*)(L + L_CNT) = 0u;
  __syncthreads();

  f32x4 acc[24];
#pragma unroll
  for (int j = 0; j < 24; ++j) acc[j] = f32x4{0.f, 0.f, 0.f, 0.f};

  const bool valid = (lane >> 5) == ((lane >> 3) & 1);
  const int part = wave * 2 + (lane >> 5);
  const int rg = (lane >> 4) & 1;

#pragma unroll
  for (int ph = 0; ph < 2; ++ph) {
    for (int it = 0; it < TPW; ++it) {
      const int i = ph * TPW + it;
      const __amdgpu_buffer_rsrc_t rn = drs(i + 1);
      const v4u a = an;
      __builtin_amdgcn_sched_barrier(0);
      an = __builtin_amdgcn_raw_buffer_load_b128(ers(i + 1), eo, 0, 0);
      __builtin_amdgcn_sched_barrier(0);
#pragma unroll
      for (int t = 0; t < 3; ++t) {
        unsigned b[8][4];
#pragma unroll
        for (int w = 0; w < 4; ++w) {
          const v4u x = ring[t * 8 + 2 * w], y = ring[t * 8 + 2 * w + 1];
#pragma unroll
          for (int c = 0; c < 4; ++c) {
            b[2 * c][w] = __builtin_amdgcn_perm(y[c], x[c], 0x05040100u);
            b[2 * c + 1][w] = __builtin_amdgcn_perm(y[c], x[c], 0x07060302u);
          }
          __builtin_amdgcn_sched_barrier(0);
          ring[t * 8 + 2 * w] = __builtin_amdgcn_raw_buffer_load_b128(rn, vo, (2 * w) * 384 + t * 128, 2);
          ring[t * 8 + 2 * w + 1] = __builtin_amdgcn_raw_buffer_load_b128(rn, vo, (2 * w + 1) * 384 + t * 128, 2);
          __builtin_amdgcn_sched_barrier(0);
        }
#pragma unroll
        for (int jj = 0; jj < 8; ++jj) {
          const v4u bb = {b[jj][0], b[jj][1], b[jj][2], b[jj][3]};
          acc[t * 8 + jj] = mfma32(a, bb, acc[t * 8 + jj]);
        }
        __builtin_amdgcn_sched_barrier(0);
      }
    }
    if (ph == 0) {
      if (valid) {
#pragma unroll
        for (int t = 0; t < 3; ++t)
#pragma unroll
          for (int i = 0; i < 4; ++i) {
            LAS f32x4* dst = (LAS f32x4*)(L + L_KP + ((part * 8 + rg * 4 + i) * PSTR + t * 64 + c8 * 8) * 4);
            dst[0] = f32x4{acc[t * 8 + 0][i], acc[t * 8 + 1][i], acc[t * 8 + 2][i], acc[t * 8 + 3][i]};
            dst[1] = f32x4{acc[t * 8 + 4][i], acc[t * 8 + 5][i], acc[t * 8 + 6][i], acc[t * 8 + 7][i]};
          }
      }
#pragma unroll
      for (int j = 0; j < 24; ++j) acc[j] = f32x4{0.f, 0.f, 0.f, 0.f};
      unsigned old = 0;
      __builtin_amdgcn_fence(__ATOMIC_RELEASE, "workgroup");
      if (lane == 0) old = __hip_atomic_fetch_add((LAS unsigned*)(L + L_CNT), 1u, __ATOMIC_RELAXED, __HIP_MEMORY_SCOPE_WORKGROUP);
      old = __builtin_amdgcn_readfirstlane(old);
      if (old == NW - 1) {
        __builtin_amdgcn_fence(__ATOMIC_ACQUIRE, "workgroup");
        const int g = lane >> 4, hb = (lane >> 3) & 1;
        f32x4 sc = {0.f, 0.f, 0.f, 0.f};
#pragma unroll
        for (int s = 0; s < 6; ++s) {
          f32x4 lo = {0.f, 0.f, 0.f, 0.f}, hi = {0.f, 0.f, 0.f, 0.f};
#pragma unroll
          for (int p = 0; p < 4; ++p) {
            const LAS f32x4* src = (const LAS f32x4*)(L + L_KP + (((hb * 4 + p) * 8 + c8) * PSTR + s * 32 + g * 8) * 4);
            lo += src[0]; hi += src[1];
          }
#pragma unroll
          for (int k = 0; k < 4; ++k) { lo[k] += dpp<0x128>(lo[k]); hi[k] += dpp<0x128>(hi[k]); }
          const v4u kb = {pk_bf16(lo[0], lo[1]), pk_bf16(lo[2], lo[3]), pk_bf16(hi[0], hi[1]), pk_bf16(hi[2], hi[3])};
          sc = mfma32(qf[s], kb, sc);
        }
        const float scale = 0.07216878364870322f;
#pragma unroll
        for (int i = 0; i < 4; ++i) {
          const float s = rbf(rbf(sc[i]) * scale);
          float m = s;
          m = fmaxf(m, dpp<0xB1>(m)); m = fmaxf(m, dpp<0x4E>(m)); m = fmaxf(m, dpp<0x141>(m));
          const float e = __expf(s - m);
          float z = e;
          z += dpp<0xB1>(z); z += dpp<0x4E>(z); z += dpp<0x141>(z);
          const float p = e / z;
          if (hb == 0) *(LAS __bf16*)(L + L_P + ((4 * g + i) * 8 + c8) * 2) = (__bf16)p;
        }
      }
    }
  }

  if (valid) {
#pragma unroll
    for (int t = 0; t < 3; ++t)
#pragma unroll
      for (int jj = 0; jj < 8; ++jj)
        *(LAS f32x4*)(L + L_VP + ((part * 2 + rg) * VSTR + t * 64 + jj * 8 + c8) * 16) = acc[t * 8 + jj];
  }
  __syncthreads();
  if (wave < 3) {
    const int kg = lane >> 4, i16 = lane & 15;
    const int dl = 16 * (i16 >> 2) + 4 * kg + (i16 & 3);
    const int u = wave * 64 + (dl & 7) * 8 + (dl >> 3);
    f32x4 lo = {0.f, 0.f, 0.f, 0.f}, hi = {0.f, 0.f, 0.f, 0.f};
#pragma unroll
    for (int p = 0; p < 8; ++p) {
      lo += *(const LAS f32x4*)(L + L_VP + ((p * 2 + 0) * VSTR + u) * 16);
      hi += *(const LAS f32x4*)(L + L_VP + ((p * 2 + 1) * VSTR + u) * 16);
    }
    const v4u va = {pk_bf16(lo[0], lo[1]), pk_bf16(lo[2], lo[3]), pk_bf16(hi[0], hi[1]), pk_bf16(hi[2], hi[3])};
    const v4u pf = *(const LAS v4u*)(L + L_P + i16 * 16);
    unsigned ow[8];
#pragma unroll
    for (int s = 0; s < 4; ++s) {
      const v4u pb = kg == s ? pf : v4u{0u, 0u, 0u, 0u};
      const f32x4 o = mfma32(va, pb, f32x4{0.f, 0.f, 0.f, 0.f});
      ow[2 * s] = pk_bf16(o[0], o[1]);
      ow[2 * s + 1] = pk_bf16(o[2], o[3]);
    }
    char* op = O + ((size_t)bh * SQ + i16) * (DH * 2) + wave * 128 + kg * 32;
    *(v4u*)op = v4u{ow[0], ow[1], ow[2], ow[3]};
    *(v4u*)(op + 16) = v4u{ow[4], ow[5], ow[6], ow[7]};
  }
}

extern "C" void kernel_launch(void* const* d_in, const int* in_sizes, int n_in,
                              void* d_out, int out_size, void* d_ws, size_t ws_size,
                              hipStream_t stream) {
  (void)in_sizes; (void)n_in; (void)out_size; (void)d_ws; (void)ws_size;
  (void)hipFuncSetAttribute((const void*)linformer_kernel, hipFuncAttributeMaxDynamicSharedMemorySize, LDS_BYTES);
  hipLaunchKernelGGL(linformer_kernel, dim3(NBH), dim3(NW * 64), LDS_BYTES, stream,
                     (const char*)d_in[0], (const char*)d_in[1], (const char*)d_in[2],
                     (const char*)d_in[3], (const char*)d_in[4], (char*)d_out);
}
```

```cpp
#include <hip/hip_runtime.h>
#include <hip/hip_bf16.h>

#define LAS __attribute__((address_space(3)))
typedef unsigned v4u __attribute__((ext_vector_type(4)));
typedef float f32x4 __attribute__((ext_vector_type(4)));
typedef float f32x2 __attribute__((ext_vector_type(2)));
typedef __bf16 bf16x8 __attribute__((ext_vector_type(8)));
typedef __bf16 bf16x2 __attribute__((ext_vector_type(2)));

constexpr int SQ = 16, SKV = 4096, DH = 192, RK = 8, NBH = 256;
constexpr int NW = 4;
constexpr int TPW = 16;
constexpr int HEAD_BYTES = SKV * DH * 2;
constexpr int PACE_K = 1740, PACE_V = 1726;
constexpr int SLOW_K = 1942, SLOW_V = 1922;
constexpr int T_FIRST = 35;
constexpr int RAMP = 15;
constexpr int PAUSE = 10;
constexpr int LAT_HI = 265, LAT_LO = 130;
constexpr int SKIP = 265;
constexpr int REPORT = 20;
constexpr int GROW_LO = 50, GROW_HI = 150;
constexpr int ISOFT = TPW + 12;
constexpr int VSTR = 200;
constexpr int PART_BYTES = 8 * 2 * VSTR * 16;
constexpr int L_PART = 0;
constexpr int L_DUMMY = 2 * PART_BYTES;
constexpr int L_Q = L_DUMMY + 4096;
constexpr int L_KB = L_Q + 6 * 1024;
constexpr int KSTR = 200;
constexpr int L_P = L_KB + 8 * KSTR * 2;
constexpr int L_FLG = L_P + 256;
constexpr int LDS_BYTES = L_FLG + 64;

__device__ __forceinline__ f32x4 mfma32(v4u a, v4u b, f32x4 c) {
  return __builtin_amdgcn_mfma_f32_16x16x32_bf16(__builtin_bit_cast(bf16x8, a), __builtin_bit_cast(bf16x8, b), c, 0, 0, 0);
}
__device__ __forceinline__ unsigned pk_bf16(float lo, float hi) {
  f32x2 v = {lo, hi};
  return __builtin_bit_cast(unsigned, __builtin_convertvector(v, bf16x2));
}
__device__ __forceinline__ float rbf(float x) { return (float)(__bf16)x; }
template <int CTRL> __device__ __forceinline__ float dpp(float x) {
  return __builtin_bit_cast(float, __builtin_amdgcn_mov_dpp(__builtin_bit_cast(int, x), CTRL, 0xf, 0xf, true));
}
__device__ __forceinline__ __amdgpu_buffer_rsrc_t rsrc(const void* p, int bytes) {
  return __builtin_amdgcn_make_buffer_rsrc((void*)p, 0, bytes, 0x00020000);
}

template <bool DUMP, int G = 0>
__device__ __forceinline__ void compute(const v4u (&ring)[24], const v4u& a, f32x4 (&acc)[24], int left, unsigned da, unsigned fa, unsigned& tdone) {
  if constexpr (G < 12) {
    if constexpr (DUMP && G == 8) {
      asm volatile("s_waitcnt vmcnt(0)" ::: "memory");
      tdone = (unsigned)__builtin_amdgcn_s_memrealtime();
    }
    constexpr int t = G / 4, c = G % 4;
    v4u b0, b1;
#pragma unroll
    for (int w = 0; w < 4; ++w) {
      const unsigned x = ring[t * 8 + 2 * w][c], y = ring[t * 8 + 2 * w + 1][c];
      b0[w] = __builtin_amdgcn_perm(y, x, 0x05040100u);
      b1[w] = __builtin_amdgcn_perm(y, x, 0x07060302u);
    }
    if constexpr (!DUMP || G == 0) {
      asm volatile("s_nop 1\n\t"
                   "v_mfma_f32_16x16x32_bf16 %0, %2, %3, %0\n\t"
                   "v_mfma_f32_16x16x32_bf16 %1, %2, %4, %1"
                   : "+a"(acc[2 * G]), "+a"(acc[2 * G + 1]) : "v"(a), "v"(b0), "v"(b1));
      if constexpr (DUMP) asm volatile("s_nop 1");
    }
    else
      asm volatile("s_cmp_lg_u32 %8, 0\n\ts_nop 0\n\t"
                   "v_mfma_f32_16x16x32_bf16 %0, %2, %3, %0\n\t"
                   "s_cbranch_scc1 1f\n\ts_nop 1\n\t"
                   "ds_write_b128 %7, %5 offset:%9\n"
                   "1:\n\tv_mfma_f32_16x16x32_bf16 %1, %2, %4, %1\n\t"
                   "s_cbranch_scc1 2f\n\t"
                   "ds_write_b128 %7, %6 offset:%10\n\t"
                   ".if %11\n\tds_write_b8 %12, %13 offset:%14\n\t.endif\n"
                   "2:"
                   : "+a"(acc[2 * G]), "+a"(acc[2 * G + 1])
                   : "v"(a), "v"(b0), "v"(b1), "a"(acc[2 * G - 2]), "a"(acc[2 * G - 1]), "v"(da), "s"(left), "i"(G * 256 - 256), "i"(G * 256 - 128),
                     "i"(c == 0 ? 1 : 0), "v"(fa), "v"(1u), "i"(t * 4 - (t ? 4 : 0))
                   : "memory", "scc");
    compute<DUMP, G + 1>(ring, a, acc, left, da, fa, tdone);
  } else {
    if constexpr (DUMP)
      asm volatile("s_cmp_lg_u32 %3, 0\n\ts_cbranch_scc1 1f\n\ts_nop 2\n\t"
                   "ds_write_b128 %2, %0 offset:2816\n\ts_nop 0\n\tds_write_b128 %2, %1 offset:2944\n\tds_write_b8 %4, %5 offset:8\n1:"
                   :: "a"(acc[22]), "a"(acc[23]), "v"(da), "s"(left), "v"(fa), "v"(1u) : "memory", "scc");
    __builtin_amdgcn_sched_barrier(0);
  }
}

__device__ __forceinline__ void zero_acc(f32x4 (&acc)[24], int skip) {
  const v4u z = {0u, 0u, 0u, 0u};
#pragma unroll
  for (int t = 0; t < 3; ++t)
    asm volatile("s_cmp_lg_u32 %9, 0\n\ts_cbranch_scc1 1f\n\ts_waitcnt lgkmcnt(0)\n\ts_nop 7\n\t"
                 "v_mfma_f32_16x16x32_bf16 %0, %8, %8, 0\n\t"
                 "v_mfma_f32_16x16x32_bf16 %1, %8, %8, 0\n\t"
                 "v_mfma_f32_16x16x32_bf16 %2, %8, %8, 0\n\t"
                 "v_mfma_f32_16x16x32_bf16 %3, %8, %8, 0\n\t"
                 "v_mfma_f32_16x16x32_bf16 %4, %8, %8, 0\n\t"
                 "v_mfma_f32_16x16x32_bf16 %5, %8, %8, 0\n\t"
                 "v_mfma_f32_16x16x32_bf16 %6, %8, %8, 0\n\t"
                 "v_mfma_f32_16x16x32_bf16 %7, %8, %8, 0\n1:"
                 : "+a"(acc[t * 8 + 0]), "+a"(acc[t * 8 + 1]), "+a"(acc[t * 8 + 2]), "+a"(acc[t * 8 + 3]),
                   "+a"(acc[t * 8 + 4]), "+a"(acc[t * 8 + 5]), "+a"(acc[t * 8 + 6]), "+a"(acc[t * 8 + 7])
                 : "v"(z), "s"(skip) : "scc");
}

__global__ void __launch_bounds__(NW * 64, 1)
linformer_kernel(const char*  , const char* __restrict__ K, const char* __restrict__ V,
                 const char* __restrict__ E, const char* __restrict__ F, char*  , char* WS) {
  extern __shared__ __attribute__((aligned(16))) char smem[];
  LAS char* L = (LAS char*)smem;
  volatile LAS unsigned* FLG = (volatile LAS unsigned*)(L + L_FLG);
  const unsigned t0 = (unsigned)__builtin_amdgcn_s_memrealtime();
  const int wj = blockIdx.x >> 3;
  const int bh = ((wj >> 3) * 8 + (blockIdx.x & 7)) * 8 + (wj & 7);
  const int lane = threadIdx.x & 63, wave = __builtin_amdgcn_readfirstlane(threadIdx.x >> 6);
  if (threadIdx.x < 15) FLG[threadIdx.x] = threadIdx.x == 8 ? t0 - 100000u : 0u;
  __syncthreads();
  const int sg = lane >> 3, c8 = lane & 7;
  const int rot = (bh >> 3) * 2 + wave;
  const char* Kh = K + (size_t)bh * HEAD_BYTES;
  const char* Vh = V + (size_t)bh * HEAD_BYTES;
  asm volatile("" : "+s"(Kh));

  const int slot = wave * 32 + wj;
  int due = T_FIRST * 128 + slot * (PACE_K * 24 / 128 + RAMP);
  const int iside = 4 + 2 * ((wj + 2 * wave) % 6);
  const int ionce = wave == 0 ? ISOFT : wave == 2 ? 9 : -9;
  int slip = 0, plat = 200, slope = 0, base = 0, look = 0, check = -1;
  unsigned tiss = t0, tdone = t0, tslot = t0;
  auto karg = [&](int n) {
    typedef char* const __attribute__((address_space(4)))* argp;
    argp p = (argp)__builtin_amdgcn_kernarg_segment_ptr();
    asm volatile("" : "+s"(p));
    return p[n];
  };
  typedef __attribute__((address_space(1))) unsigned long long gu64;
  auto report = [&](unsigned when, unsigned what) {
    int b = blockIdx.x, l = lane;
    asm volatile("" : "+s"(b));
    asm volatile("" : "+v"(l));
    if ((b & 24) == 8 && l < 8)
      __hip_atomic_store((gu64*)WS + (l * 544 + (b >> 5) * 8 + (b & 7)), when | ((unsigned long long)what << 32), __ATOMIC_RELAXED, __HIP_MEMORY_SCOPE_AGENT);
  };
  auto reports = [&]() {
    int b = blockIdx.x;
    asm volatile("" : "+s"(b));
    return __hip_atomic_load((gu64*)WS + ((b & 7) * 544 + lane), __ATOMIC_RELAXED, __HIP_MEMORY_SCOPE_AGENT);
  };
  auto burst = [&](int i, v4u (&ring)[24], v4u& a) {
    const int t = (rot + NW * i) & 63;
    const int pr = (t & 15) * 4 + (sg & 3);
    int g = pr + (pr >= 21 ? 22 : 0) + (pr >= 42 ? 22 : 0) + (sg >> 2) * 21;
    if (pr == 63) g = 42 + (sg >> 2) * 43;
    g += (t >> 4) * 128;
    const int vo = (g * 3072 + c8 * 16) & 0x1fffff, eo = c8 * (SKV * 2) + g * 16;
    const __amdgpu_buffer_rsrc_t rd = rsrc(i < TPW ? Kh : Vh, HEAD_BYTES);
    const __amdgpu_buffer_rsrc_t re = rsrc(i < TPW ? E : F, RK * SKV * 2);
    const int k = i & (TPW - 1);
    if (i > 1) {
      if (!(i & 1)) {
        asm volatile("s_waitcnt vmcnt(0)" ::: "memory");
        tdone = (unsigned)__builtin_amdgcn_s_memrealtime();
      }
      const unsigned now = tdone;
      const int lat = (i == iside + 2 || (!(i & 1) && i == ionce + 2)) ? plat : (int)(now - tiss) - (i == TPW + 1 ? 30 : 0);
      if (lat > LAT_HI && plat < (i < 5 ? LAT_LO + 30 : LAT_LO)) {
        const int cs = __builtin_amdgcn_readfirstlane(FLG[7]), since = __builtin_amdgcn_readfirstlane((int)(now - FLG[8]));
        if (cs == slip && since > 3000) {
          check = i + 1;
          FLG[7] = cs + SKIP; FLG[8] = now; FLG[11] = 1u;
          report(now, ~0u);
        }
      } else if (i == check) {
        const unsigned long long vote = reports();
        const int n = __builtin_popcountll(__builtin_amdgcn_ballot_w64(now + 200u - (unsigned)vote < 1500u && (int)(vote >> 32) < 0));
        if (n < 16 && FLG[11]) { FLG[7] = FLG[7] - SKIP; FLG[11] = 0u; }
      }
      plat = lat;
      if (k == 2 || k == 5 || k == 11) {
        const int e = min(max((int)(now - tslot) - (k == 2 ? 100 : 125), 0), 1000);
        unsigned sum = 0;
        if (lane == 0) sum = __hip_atomic_fetch_add((LAS unsigned*)(L + L_FLG) + 10, (unsigned)e, __ATOMIC_RELAXED, __HIP_MEMORY_SCOPE_WORKGROUP);
        if (wave == NW - 1) {
          const int m = min((__builtin_amdgcn_readfirstlane(sum) + e) >> 2, 1000);
          FLG[10] = 0u;
          if (k == 2) base = m;
          else {
            const int since = __builtin_amdgcn_readfirstlane((int)(now - FLG[8]));
            look = 1;
            if (m > REPORT && since > 1000) report(now, m | (base << 16));
          }
        }
      } else if (look && (k == 6 || k == 7 || (k == 12 && (i < TPW || slope == 0)))) {
        const unsigned long long vote = reports();
        const bool fresh = now + 200u - (unsigned)vote < 1300u && (int)(vote >> 32) >= 0;
        const int n = __builtin_popcountll(__builtin_amdgcn_ballot_w64(fresh));
        if (n < 4) look = 0;
        if (n >= 16) {
          int c = fresh ? (int)(vote >> 32) : 0;
          c += __builtin_amdgcn_mov_dpp(c, 0xB1, 0xf, 0xf, true); c += __builtin_amdgcn_mov_dpp(c, 0x4E, 0xf, 0xf, true);
          c += __builtin_amdgcn_mov_dpp(c, 0x141, 0xf, 0xf, true); c += __builtin_amdgcn_mov_dpp(c, 0x140, 0xf, 0xf, true);
          const unsigned cs = (unsigned)(__builtin_amdgcn_readlane(c, 0) + __builtin_amdgcn_readlane(c, 16) + __builtin_amdgcn_readlane(c, 32) + __builtin_amdgcn_readlane(c, 48));
          const int lev = cs & 0xffffu, gro = lev - (int)(cs >> 16);
          const bool local = (int)(now - FLG[8]) < (k < 8 ? 2400 : 1700) && FLG[11];
          if ((n >= 40 || (k != 6 && !local)) && lev >= 15 * 64) {
            int ns = FLG[7];
            if (local) ns -= SKIP;
            if (k < 8 && n >= 40 && gro >= GROW_LO * n && gro < GROW_HI * n) {
              FLG[12 + (i >> 4)] = 1u;
              ns += max((int)(cs >> 22) - 62, 0);
            } else ns += ((lev * 20) >> 10) + 20;
            FLG[7] = ns;
            FLG[8] = now - 2000u;
            FLG[11] = 0u;
            look = 0;
          }
        }
      }
    }
    if ((i & 1) && i == ionce) {
      int b = blockIdx.x;
      asm volatile("" : "+s"(b));
      int dt = (int)(t0 - (unsigned)__hip_atomic_load((gu64*)WS + ((b & 7) * 544 + 64 + (lane & 7)), __ATOMIC_RELAXED, __HIP_MEMORY_SCOPE_AGENT));
      if (__builtin_amdgcn_ballot_w64(dt < -600 || dt > 600) == 0) {
        dt += __builtin_amdgcn_mov_dpp(dt, 0xB1, 0xf, 0xf, true); dt += __builtin_amdgcn_mov_dpp(dt, 0x4E, 0xf, 0xf, true);
        dt += __builtin_amdgcn_mov_dpp(dt, 0x141, 0xf, 0xf, true);
        FLG[14] = (dt * 3) >> 2;
      }
    }
    if (i > 10) due -= __builtin_amdgcn_readfirstlane(FLG[14]);
    if (k > 5 && slope == 0 && __builtin_amdgcn_readfirstlane(FLG[12 + (i >> 4)])) {
      slope = (i < TPW ? SLOW_K - PACE_K : SLOW_V - PACE_V) * 24; due += slope * k + ((slope * slot) >> 7); plat = 200;
    }
    if (i == TPW) { due -= (slope * slot) >> 7; slope = 0; }
    const unsigned d = t0 + (unsigned)(due >> 7);
    int over, tmp;
    asm volatile("s_branch 2f\n"
                 "1:\n\ts_sleep 1\n"
                 "2:\n\tds_read_b32 %2, %4\n\ts_memrealtime vcc\n\ts_waitcnt lgkmcnt(0)\n\tv_readfirstlane_b32 %1, %2\n\t"
                 "s_sub_u32 %0, vcc_lo, %3\n\ts_cmp_lt_i32 %0, %1\n\ts_cbranch_scc1 1b"
                 : "=&s"(over), "=&s"(slip), "=&v"(tmp) : "s"(d), "v"(&FLG[7]), "v"(vo), "v"(eo) : "vcc", "scc", "memory");
    tiss = d + (unsigned)over;
    tslot = d + (unsigned)slip;
    due += (i < TPW ? PACE_K : PACE_V) * 24 + slope +
           (i == TPW - 1 ? PAUSE * 128 - (((PACE_K - PACE_V) * 24 * slot) >> 7) : i == 0 ? (128 - slot) * RAMP : 0);
    a = __builtin_amdgcn_raw_buffer_load_b128(re, eo, 0, 0);
    __builtin_amdgcn_sched_barrier(0);
#pragma unroll
    for (int j = 0; j < 24; ++j) {
      ring[j] = __builtin_amdgcn_raw_buffer_load_b128(rd, vo + (j & 7) * 384 + (j >> 3) * 128, 0, 2);
      __builtin_amdgcn_sched_barrier(0);
    }
  };

  if (wj == 31 && wave == NW - 1 && lane < 8)
    __hip_atomic_store((gu64*)WS + (lane * 544 + 64 + (blockIdx.x & 7)), (unsigned long long)t0, __ATOMIC_RELAXED, __HIP_MEMORY_SCOPE_AGENT);
  v4u r0[24], r1[24], a0, a1;
  burst(0, r0, a0);
  asm volatile("" : "+s"(Vh));

  const bool valid = (lane >> 5) == (sg & 1);
  const unsigned dumpa = (unsigned)(size_t)L + (valid ? L_PART + (((wave * 2 + (lane >> 5)) * 2 + ((lane >> 4) & 1)) * VSTR + c8) * 16 : L_DUMMY + lane * 16);
  const unsigned dumps = valid ? PART_BYTES : 0;
  const unsigned flaga = (unsigned)(size_t)(L + L_FLG) + wave;
  const int kg = lane >> 4, i16 = lane & 15;
  const int dl = 16 * (i16 >> 2) + 4 * kg + (i16 & 3);
  const int redo = ((dl & 7) * 8 + (dl >> 3)) * 16;
  auto ld4 = [&](int off) { return *(const LAS f32x4*)(L + off); };
  auto wait_flag = [&](int idx, unsigned mask) {
    while ((FLG[idx] & mask) != mask) __builtin_amdgcn_s_sleep(0);
    __builtin_amdgcn_fence(__ATOMIC_ACQUIRE, "workgroup");
  };

  f32x4 acc[24];
#pragma unroll
  for (int j = 0; j < 24; ++j) asm volatile("" : "=a"(acc[j]));
  zero_acc(acc, 0);
  int ih = 0;
#pragma unroll 1
  for (int ph = 0; ph < 2; ++ph) {
    const int hend = ph ? TPW : TPW / 2 + 5;
#pragma unroll 1
    do {
      const int i = 2 * ih;
      burst(i + 1, r1, a1);
      if (i == iside) {
        if (wave < 3) {
          int hq = bh;
          asm volatile("" : "+s"(hq));
          const __amdgpu_buffer_rsrc_t rq = rsrc(karg(0) + (size_t)hq * (SQ * DH * 2), SQ * DH * 2);
          const int qo = lane * 16 + wave * 2048;
          const unsigned ql = (unsigned)(size_t)(L + L_Q) + wave * 2048;
          unsigned keep;
          asm volatile("s_mov_b32 %0, m0\n\ts_mov_b32 m0, %3\n\ts_nop 4\n\tbuffer_load_dwordx4 %1, %2, 0 offen nt lds\n\t"
                       "buffer_load_dwordx4 %1, %2, 0 offen offset:1024 nt lds\n\ts_mov_b32 m0, %0"
                       : "=&s"(keep) : "v"(qo), "s"(rq), "s"(ql) : "memory");
        } else {
          int hq = bh;
          asm volatile("" : "+s"(hq));
          const unsigned x = *(const unsigned*)(F + (wj * 16 + i16) * 128);
          int lq = lane;
          asm volatile("" : "+v"(lq));
          typedef const __attribute__((address_space(1))) unsigned gu32;
          const unsigned y = __builtin_nontemporal_load((gu32*)(Vh + (lq & 1) * (HEAD_BYTES - 4)));
          const unsigned z = __builtin_nontemporal_load((gu32*)(karg(5) + (size_t)hq * (SQ * DH * 2) + (lq & 1) * (SQ * DH * 2 - 4)));
          asm volatile("" :: "v"(x), "v"(y), "v"(z));
        }
      }
      compute<false>(r0, a0, acc, 0, 0u, 0u, tdone);
      if (i == ionce) {
        wait_flag(9, 0x00010101u);
        f32x4 sc = {0.f, 0.f, 0.f, 0.f};
#pragma unroll
        for (int s = 0; s < 6; ++s) {
          const v4u qf = *(const LAS v4u*)(L + L_Q + i16 * (DH * 2) + s * 64 + kg * 16);
          const v4u kb = *(const LAS v4u*)(L + L_KB + (c8 * KSTR + s * 32 + kg * 8) * 2);
          sc = mfma32(qf, kb, sc);
        }
        const float scale = 0.07216878364870322f;
#pragma unroll
        for (int j = 0; j < 4; ++j) {
          const float s = rbf(rbf(sc[j]) * scale);
          float m = s;
          m = fmaxf(m, dpp<0xB1>(m)); m = fmaxf(m, dpp<0x4E>(m)); m = fmaxf(m, dpp<0x141>(m));
          const float e = __expf(s - m);
          float z = e;
          z += dpp<0xB1>(z); z += dpp<0x4E>(z); z += dpp<0x141>(z);
          const float p = e * __builtin_amdgcn_rcpf(z);
          *(LAS __bf16*)(L + L_P + ((4 * kg + j) * 8 + c8) * 2) = (__bf16)p;
        }
        __builtin_amdgcn_fence(__ATOMIC_RELEASE, "workgroup");
        FLG[6] = 1u;
      }
      if (i + 2 < 2 * TPW) burst(i + 2, r0, a0);
      compute<true>(r1, a1, acc, (i + 2) & (TPW - 1), dumpa + (i >> 4) * dumps, flaga + (i >> 4) * 12, tdone);
      zero_acc(acc, i - (TPW - 2));
    } while (++ih < hend);
    if (wave < 3) {
      int ho = bh, kq = kg;
      char* const Op = karg(5);
      asm volatile("" : "+s"(ho));
      asm volatile("" : "+v"(kq));
      wait_flag(6, (unsigned)ph);
      const v4u pf = *(const LAS v4u*)(L + L_P + i16 * 16);
      v4u pb[4];
#pragma unroll
      for (int s = 0; s < 4; ++s) pb[s] = kq == s ? pf : v4u{0u, 0u, 0u, 0u};
      const int off = L_PART + ph * PART_BYTES + wave * 1024 + redo;
      wait_flag(ph * 3 + wave, 0x00010101u);
      f32x4 lo = ld4(off), hi = ld4(off + VSTR * 16);
#pragma unroll
      for (int p = 1; p < 6; ++p) { lo += ld4(off + (p * 2) * VSTR * 16); hi += ld4(off + (p * 2 + 1) * VSTR * 16); }
      asm volatile("" : "+v"(lo), "+v"(hi), "+v"(pb[0]), "+v"(pb[1]), "+v"(pb[2]), "+v"(pb[3]));
      wait_flag(ph * 3 + wave, 0x01010101u);
      const f32x4 l6 = ld4(off + 12 * VSTR * 16), h6 = ld4(off + 13 * VSTR * 16), l7 = ld4(off + 14 * VSTR * 16), h7 = ld4(off + 15 * VSTR * 16);
      lo = (lo + l6) + l7; hi = (hi + h6) + h7;
      const v4u va = {pk_bf16(lo[0], lo[1]), pk_bf16(lo[2], lo[3]), pk_bf16(hi[0], hi[1]), pk_bf16(hi[2], hi[3])};
      f32x4 o0, o1, o2, o3;
      asm volatile("s_nop 1\n\t"
                   "v_mfma_f32_16x16x32_bf16 %0, %4, %5, 0\n\t"
                   "v_mfma_f32_16x16x32_bf16 %1, %4, %6, 0\n\t"
                   "v_mfma_f32_16x16x32_bf16 %2, %4, %7, 0\n\t"
                   "v_mfma_f32_16x16x32_bf16 %3, %4, %8, 0\n\t"
                   "s_nop 7"
                   : "=&v"(o0), "=&v"(o1), "=&v"(o2), "=&v"(o3) : "v"(va), "v"(pb[0]), "v"(pb[1]), "v"(pb[2]), "v"(pb[3]));
      const __amdgpu_buffer_rsrc_t ro = rsrc(Op + (size_t)ho * (SQ * DH * 2), ph ? SQ * DH * 2 : 0);
      const int oo = i16 * (DH * 2) + wave * 128 + kg * 32;
      __builtin_amdgcn_raw_buffer_store_b128(v4u{pk_bf16(o0[0], o0[1]), pk_bf16(o0[2], o0[3]), pk_bf16(o1[0], o1[1]), pk_bf16(o1[2], o1[3])}, ro, oo, 0, 0);
      __builtin_amdgcn_raw_buffer_store_b128(v4u{pk_bf16(o2[0], o2[1]), pk_bf16(o2[2], o2[3]), pk_bf16(o3[0], o3[1]), pk_bf16(o3[2], o3[3])}, ro, oo, 16, 0);
      if (ph == 0) {
        LAS unsigned short* dst = (LAS unsigned short*)(L + L_KB) + wave * 64 + dl;
#pragma unroll
        for (int k = 0; k < 4; ++k) { dst[2 * k * KSTR] = (unsigned short)(va[k] & 0xffffu); dst[(2 * k + 1) * KSTR] = (unsigned short)(va[k] >> 16); }
        __builtin_amdgcn_fence(__ATOMIC_RELEASE, "workgroup");
        *((volatile LAS unsigned char*)&FLG[9] + wave) = 1;
      }
    } else if (ph == 0) {
      int hq = bh, lq = lane;
      asm volatile("" : "+s"(hq));
      asm volatile("" : "+v"(lq));
      typedef const __attribute__((address_space(1))) unsigned gu32;
      const unsigned z = __builtin_nontemporal_load((gu32*)(karg(5) + (size_t)hq * (SQ * DH * 2) + (lq & 1) * (SQ * DH * 2 - 4)));
      asm volatile("" :: "v"(z));
    }
  }
}

extern "C" void kernel_launch(void* const* d_in, const int* in_sizes, int n_in,
                              void* d_out, int out_size, void* d_ws, size_t ws_size,
                              hipStream_t stream) {
  (void)in_sizes; (void)n_in; (void)out_size; (void)ws_size;
  (void)hipFuncSetAttribute((const void*)linformer_kernel, hipFuncAttributeMaxDynamicSharedMemorySize, LDS_BYTES);
  hipLaunchKernelGGL(linformer_kernel, dim3(NBH), dim3(NW * 64), LDS_BYTES, stream,
                     (const char*)d_in[0], (const char*)d_in[1], (const char*)d_in[2],
                     (const char*)d_in[3], (const char*)d_in[4], (char*)d_out, (char*)d_ws);
}
```
